```python
import torch

def _softmax(x, axis=-1):
    return torch.softmax(x, dim=axis)
def _gelu(x):
    return torch.nn.functional.gelu(x, approximate="tanh")
def _silu(x):
    return torch.nn.functional.silu(x)
def _sigmoid(x):
    return torch.sigmoid(x)
def _layernorm(x, axis=-1, eps=1e-5):
    m = x.mean(dim=axis, keepdim=True)
    v = x.var(dim=axis, keepdim=True, unbiased=False)
    return (x - m) / torch.sqrt(v + eps)
def _scatter_add(tgt, idx, src, axis=0):
    return tgt.clone().index_add_(axis, idx, src)
def _segment_sum(src, seg, n):
    out = torch.zeros((n,) + src.shape[1:], dtype=src.dtype)
    return out.index_add_(0, seg, src)
def _pad(x, pw):
    flat = tuple(v for pair in reversed(pw) for v in pair)
    return torch.nn.functional.pad(x, flat)

kernel_name = 'standalone__deep_cross_wide_deep__recsys'

def setup_inputs():

    _w = (lambda a: (lambda t: t.to(torch.bfloat16) if t.is_floating_point() else t)(torch.as_tensor(a)))
    B, F, D = 16384, 26, 128
    V, Nd = min(1000000, 100000), 13
    Din = F * D
    H = 4 * D
    return {
        "idx": _w((lambda lo, hi, *s: torch.randint(lo, hi, s))(0, V, B, F).to(torch.int64)),
        "table": _w((lambda *s: torch.randn(*s, dtype=torch.bfloat16) * 1.6)(V, D) * 0.05),
        "x_wide": _w((lambda *s: torch.randn(*s, dtype=torch.bfloat16) * 1.6)(B, Nd)),
        "w_wide": _w((lambda *s: torch.randn(*s, dtype=torch.bfloat16) * 1.6)(Nd) * 0.05),
        "b_wide": _w((lambda *s: torch.randn(*s, dtype=torch.bfloat16) * 1.6)(1) * 0.01),
        "W1": _w((lambda *s: torch.randn(*s, dtype=torch.bfloat16) * 1.6)(Din, H) * (2.0 / Din) ** 0.5),
        "b1": _w((lambda *s: torch.randn(*s, dtype=torch.bfloat16) * 1.6)(H) * 0.01),
        "W2": _w((lambda *s: torch.randn(*s, dtype=torch.bfloat16) * 1.6)(H) * (1.0 / H) ** 0.5),
        "b2": _w((lambda *s: torch.randn(*s, dtype=torch.bfloat16) * 1.6)(1) * 0.01),
    }

def reference(idx, table, x_wide, w_wide, b_wide, W1, b1, W2, b2):
    emb = table[idx]
    deep_in = emb.reshape(emb.shape[0], -1)
    h = deep_in @ W1 + b1
    h = h * (lambda x, dt: x.to(dt))(h > 0, h.dtype)
    deep_logit = h @ W2 + b2
    wide_logit = x_wide @ w_wide + b_wide
    return _sigmoid(wide_logit + deep_logit)
```

```cpp
#include <hip/hip_runtime.h>
#include <hip/hip_bf16.h>

#define LAS __attribute__((address_space(3)))
#define GAS __attribute__((address_space(1)))
typedef unsigned long long u64x8 __attribute__((ext_vector_type(8)));
typedef unsigned long long u64x4 __attribute__((ext_vector_type(4)));
typedef float f32x4 __attribute__((ext_vector_type(4)));
typedef __attribute__((address_space(1))) unsigned long long gu64;

constexpr int TDIST = 2;
constexpr int B_ = 16384, F_ = 26, D_ = 128, ND_ = 13;
constexpr int A_SLOT = 32768, B_BASE = 65536, B_SLOT = 32768, LDS_BYTES = 163840;

__device__ __forceinline__ float bf2f(unsigned short u) { return __uint_as_float(((unsigned)u) << 16); }
__device__ __forceinline__ float rbf(float x) { return __bfloat162float(__float2bfloat16(x)); }
template <int N> __device__ __forceinline__ void waitv() { asm volatile("s_waitcnt vmcnt(%0)" ::"i"(N) : "memory"); }
__device__ __forceinline__ void bar() { asm volatile("s_barrier" ::: "memory"); }

__device__ __forceinline__ void dma4(const char* sb, unsigned m0v, unsigned v0, unsigned v1, unsigned v2, unsigned v3) {
  asm volatile("s_mov_b32 m0, %0\n\ts_nop 0\n\t"
               "global_load_lds_dwordx4 %1, %5\n\t"
               "global_load_lds_dwordx4 %2, %5 offset:1024\n\t"
               "global_load_lds_dwordx4 %3, %5 offset:2048\n\t"
               "global_load_lds_dwordx4 %4, %5 offset:3072"
               ::"s"(m0v), "v"(v0), "v"(v1), "v"(v2), "v"(v3), "s"(sb) : "memory");
}
template <int H> __device__ __forceinline__ void dma4h(const char* sb, unsigned m0v, unsigned v0, unsigned v1, unsigned v2, unsigned v3) {
  asm volatile("s_mov_b32 m0, %0\n\ts_mov_b32 exec_lo, %6\n\ts_mov_b32 exec_hi, %6\n\t"
               "global_load_lds_dwordx4 %1, %5\n\t"
               "global_load_lds_dwordx4 %2, %5 offset:1024\n\t"
               "s_not_b64 exec, exec\n\t"
               "global_load_lds_dwordx4 %3, %5 offset:2048\n\t"
               "global_load_lds_dwordx4 %4, %5 offset:3072\n\t"
               "s_mov_b64 exec, -1"
               ::"s"(m0v), "v"(v0), "v"(v1), "v"(v2), "v"(v3), "s"(sb), "i"(H ? 0xFF00FF00u : 0x00FF00FFu) : "memory", "scc");
}
template <int J> __device__ __forceinline__ unsigned bcast16(unsigned x) {
  return (unsigned)__builtin_amdgcn_update_dpp(0, (int)x, 0x150 + J, 0xf, 0xf, false);
}

#define SCLOB "s64", "s65", "s66", "s67", "s68", "s69", "s70", "s71", "s72", "s73", "s74", "s75", "s76", "s77", "s78", "s79", "s80", "s81", "s82", "s83", "s84", "s85", "s86", "s87", "s88", "s89", "s90", "s91", "s92", "s93", "s94", "s95"
#define CONSUMER_ASM \
  "v_xor_b32 v4, 0x0, %[a0]\n\t" \
  "v_xor_b32 v5, 0x40, %[a0]\n\t" \
  "v_xor_b32 v6, 0x80, %[a0]\n\t" \
  "v_xor_b32 v7, 0xc0, %[a0]\n\t" \
  "v_xor_b32 v8, 0x0, %[b0]\n\t" \
  "v_xor_b32 v9, 0x20, %[b0]\n\t" \
  "v_xor_b32 v10, 0x40, %[b0]\n\t" \
  "v_xor_b32 v11, 0x60, %[b0]\n\t" \
  "v_mov_b32 v12, 0xffff0000\n\t" \
  "s_waitcnt lgkmcnt(0)\n\t" \
  "s_mov_b32 exec_lo, 0xffff\n\t" \
  "s_mov_b32 exec_hi, 0\n\t" \
  "v_lshlrev_b32_e64 v128, 16, s64\n\t" \
  "v_and_b32_e32 v129, s64, v12\n\t" \
  "v_lshlrev_b32_e64 v130, 16, s65\n\t" \
  "v_and_b32_e32 v131, s65, v12\n\t" \
  "v_lshlrev_b32_e64 v132, 16, s72\n\t" \
  "v_and_b32_e32 v133, s72, v12\n\t" \
  "v_lshlrev_b32_e64 v134, 16, s73\n\t" \
  "v_and_b32_e32 v135, s73, v12\n\t" \
  "v_lshlrev_b32_e64 v136, 16, s80\n\t" \
  "v_and_b32_e32 v137, s80, v12\n\t" \
  "v_lshlrev_b32_e64 v138, 16, s81\n\t" \
  "v_and_b32_e32 v139, s81, v12\n\t" \
  "v_lshlrev_b32_e64 v140, 16, s88\n\t" \
  "v_and_b32_e32 v141, s88, v12\n\t" \
  "v_lshlrev_b32_e64 v142, 16, s89\n\t" \
  "v_and_b32_e32 v143, s89, v12\n\t" \
  "s_mov_b32 exec_lo, 0xffff0000\n\t" \
  "s_mov_b32 exec_hi, 0\n\t" \
  "v_lshlrev_b32_e64 v128, 16, s66\n\t" \
  "v_and_b32_e32 v129, s66, v12\n\t" \
  "v_lshlrev_b32_e64 v130, 16, s67\n\t" \
  "v_and_b32_e32 v131, s67, v12\n\t" \
  "v_lshlrev_b32_e64 v132, 16, s74\n\t" \
  "v_and_b32_e32 v133, s74, v12\n\t" \
  "v_lshlrev_b32_e64 v134, 16, s75\n\t" \
  "v_and_b32_e32 v135, s75, v12\n\t" \
  "v_lshlrev_b32_e64 v136, 16, s82\n\t" \
  "v_and_b32_e32 v137, s82, v12\n\t" \
  "v_lshlrev_b32_e64 v138, 16, s83\n\t" \
  "v_and_b32_e32 v139, s83, v12\n\t" \
  "v_lshlrev_b32_e64 v140, 16, s90\n\t" \
  "v_and_b32_e32 v141, s90, v12\n\t" \
  "v_lshlrev_b32_e64 v142, 16, s91\n\t" \
  "v_and_b32_e32 v143, s91, v12\n\t" \
  "s_mov_b32 exec_lo, 0\n\t" \
  "s_mov_b32 exec_hi, 0xffff\n\t" \
  "v_lshlrev_b32_e64 v128, 16, s68\n\t" \
  "v_and_b32_e32 v129, s68, v12\n\t" \
  "v_lshlrev_b32_e64 v130, 16, s69\n\t" \
  "v_and_b32_e32 v131, s69, v12\n\t" \
  "v_lshlrev_b32_e64 v132, 16, s76\n\t" \
  "v_and_b32_e32 v133, s76, v12\n\t" \
  "v_lshlrev_b32_e64 v134, 16, s77\n\t" \
  "v_and_b32_e32 v135, s77, v12\n\t" \
  "v_lshlrev_b32_e64 v136, 16, s84\n\t" \
  "v_and_b32_e32 v137, s84, v12\n\t" \
  "v_lshlrev_b32_e64 v138, 16, s85\n\t" \
  "v_and_b32_e32 v139, s85, v12\n\t" \
  "v_lshlrev_b32_e64 v140, 16, s92\n\t" \
  "v_and_b32_e32 v141, s92, v12\n\t" \
  "v_lshlrev_b32_e64 v142, 16, s93\n\t" \
  "v_and_b32_e32 v143, s93, v12\n\t" \
  "s_mov_b32 exec_lo, 0\n\t" \
  "s_mov_b32 exec_hi, 0xffff0000\n\t" \
  "v_lshlrev_b32_e64 v128, 16, s70\n\t" \
  "v_and_b32_e32 v129, s70, v12\n\t" \
  "v_lshlrev_b32_e64 v130, 16, s71\n\t" \
  "v_and_b32_e32 v131, s71, v12\n\t" \
  "v_lshlrev_b32_e64 v132, 16, s78\n\t" \
  "v_and_b32_e32 v133, s78, v12\n\t" \
  "v_lshlrev_b32_e64 v134, 16, s79\n\t" \
  "v_and_b32_e32 v135, s79, v12\n\t" \
  "v_lshlrev_b32_e64 v136, 16, s86\n\t" \
  "v_and_b32_e32 v137, s86, v12\n\t" \
  "v_lshlrev_b32_e64 v138, 16, s87\n\t" \
  "v_and_b32_e32 v139, s87, v12\n\t" \
  "v_lshlrev_b32_e64 v140, 16, s94\n\t" \
  "v_and_b32_e32 v141, s94, v12\n\t" \
  "v_lshlrev_b32_e64 v142, 16, s95\n\t" \
  "v_and_b32_e32 v143, s95, v12\n\t" \
  "s_mov_b64 exec, -1\n\t" \
  "v_mov_b64 v[144:145], v[128:129]\n\t" \
  "v_mov_b64 v[146:147], v[130:131]\n\t" \
  "v_mov_b64 v[148:149], v[132:133]\n\t" \
  "v_mov_b64 v[150:151], v[134:135]\n\t" \
  "v_mov_b64 v[152:153], v[136:137]\n\t" \
  "v_mov_b64 v[154:155], v[138:139]\n\t" \
  "v_mov_b64 v[156:157], v[140:141]\n\t" \
  "v_mov_b64 v[158:159], v[142:143]\n\t" \
  "v_mov_b64 v[160:161], v[128:129]\n\t" \
  "v_mov_b64 v[162:163], v[130:131]\n\t" \
  "v_mov_b64 v[164:165], v[132:133]\n\t" \
  "v_mov_b64 v[166:167], v[134:135]\n\t" \
  "v_mov_b64 v[168:169], v[136:137]\n\t" \
  "v_mov_b64 v[170:171], v[138:139]\n\t" \
  "v_mov_b64 v[172:173], v[140:141]\n\t" \
  "v_mov_b64 v[174:175], v[142:143]\n\t" \
  "v_mov_b64 v[176:177], v[128:129]\n\t" \
  "v_mov_b64 v[178:179], v[130:131]\n\t" \
  "v_mov_b64 v[180:181], v[132:133]\n\t" \
  "v_mov_b64 v[182:183], v[134:135]\n\t" \
  "v_mov_b64 v[184:185], v[136:137]\n\t" \
  "v_mov_b64 v[186:187], v[138:139]\n\t" \
  "v_mov_b64 v[188:189], v[140:141]\n\t" \
  "v_mov_b64 v[190:191], v[142:143]\n\t" \
  "v_mov_b64 v[192:193], v[128:129]\n\t" \
  "v_mov_b64 v[194:195], v[130:131]\n\t" \
  "v_mov_b64 v[196:197], v[132:133]\n\t" \
  "v_mov_b64 v[198:199], v[134:135]\n\t" \
  "v_mov_b64 v[200:201], v[136:137]\n\t" \
  "v_mov_b64 v[202:203], v[138:139]\n\t" \
  "v_mov_b64 v[204:205], v[140:141]\n\t" \
  "v_mov_b64 v[206:207], v[142:143]\n\t" \
  "v_mov_b64 v[208:209], v[128:129]\n\t" \
  "v_mov_b64 v[210:211], v[130:131]\n\t" \
  "v_mov_b64 v[212:213], v[132:133]\n\t" \
  "v_mov_b64 v[214:215], v[134:135]\n\t" \
  "v_mov_b64 v[216:217], v[136:137]\n\t" \
  "v_mov_b64 v[218:219], v[138:139]\n\t" \
  "v_mov_b64 v[220:221], v[140:141]\n\t" \
  "v_mov_b64 v[222:223], v[142:143]\n\t" \
  "v_mov_b64 v[224:225], v[128:129]\n\t" \
  "v_mov_b64 v[226:227], v[130:131]\n\t" \
  "v_mov_b64 v[228:229], v[132:133]\n\t" \
  "v_mov_b64 v[230:231], v[134:135]\n\t" \
  "v_mov_b64 v[232:233], v[136:137]\n\t" \
  "v_mov_b64 v[234:235], v[138:139]\n\t" \
  "v_mov_b64 v[236:237], v[140:141]\n\t" \
  "v_mov_b64 v[238:239], v[142:143]\n\t" \
  "v_mov_b64 v[240:241], v[128:129]\n\t" \
  "v_mov_b64 v[242:243], v[130:131]\n\t" \
  "v_mov_b64 v[244:245], v[132:133]\n\t" \
  "v_mov_b64 v[246:247], v[134:135]\n\t" \
  "v_mov_b64 v[248:249], v[136:137]\n\t" \
  "v_mov_b64 v[250:251], v[138:139]\n\t" \
  "v_mov_b64 v[252:253], v[140:141]\n\t" \
  "v_mov_b64 v[254:255], v[142:143]\n\t" \
  "s_barrier\n\t" \
  "global_load_dwordx2 v[16:17], %[vo], %[pw2] offset:0\n\t" \
  "global_load_dwordx2 v[18:19], %[vo], %[pw2] offset:32\n\t" \
  "global_load_dwordx2 v[20:21], %[vo], %[pw2] offset:64\n\t" \
  "global_load_dwordx2 v[22:23], %[vo], %[pw2] offset:96\n\t" \
  "ds_read_b128 v[32:35], v4 offset:0\n\t" \
  "ds_read_b128 v[36:39], v5 offset:0\n\t" \
  "ds_read_b128 v[40:43], v4 offset:4096\n\t" \
  "ds_read_b128 v[44:47], v5 offset:4096\n\t" \
  "ds_read_b128 v[48:51], v4 offset:8192\n\t" \
  "ds_read_b128 v[52:55], v5 offset:8192\n\t" \
  "ds_read_b64_tr_b16 v[96:97], v8 offset:0\n\t" \
  "ds_read_b64_tr_b16 v[98:99], v8 offset:4096\n\t" \
  "ds_read_b64_tr_b16 v[100:101], v8 offset:16384\n\t" \
  "ds_read_b64_tr_b16 v[102:103], v8 offset:20480\n\t" \
  "ds_read_b128 v[56:59], v4 offset:12288\n\t" \
  "ds_read_b128 v[60:63], v5 offset:12288\n\t" \
  "ds_read_b64_tr_b16 v[104:105], v9 offset:0\n\t" \
  "ds_read_b64_tr_b16 v[106:107], v9 offset:4096\n\t" \
  "ds_read_b64_tr_b16 v[108:109], v9 offset:16384\n\t" \
  "ds_read_b64_tr_b16 v[110:111], v9 offset:20480\n\t" \
  "ds_read_b64_tr_b16 v[112:113], v10 offset:0\n\t" \
  "ds_read_b64_tr_b16 v[114:115], v10 offset:4096\n\t" \
  "ds_read_b64_tr_b16 v[116:117], v10 offset:16384\n\t" \
  "ds_read_b64_tr_b16 v[118:119], v10 offset:20480\n\t" \
  "s_waitcnt lgkmcnt(4)\n\t" \
  ".p2align 6\n\t" \
  "1:\n\t" \
  "v_mfma_f32_16x16x32_bf16 v[128:131], v[96:99], v[32:35], v[128:131]\n\t" \
  "ds_read_b64_tr_b16 v[120:121], v11 offset:0\n\t" \
  "v_mfma_f32_16x16x32_bf16 v[128:131], v[100:103], v[36:39], v[128:131]\n\t" \
  "ds_read_b64_tr_b16 v[122:123], v11 offset:4096\n\t" \
  "v_mfma_f32_16x16x32_bf16 v[144:147], v[100:103], v[44:47], v[144:147]\n\t" \
  "ds_read_b64_tr_b16 v[124:125], v11 offset:16384\n\t" \
  "v_mfma_f32_16x16x32_bf16 v[144:147], v[96:99], v[40:43], v[144:147]\n\t" \
  "ds_read_b64_tr_b16 v[126:127], v11 offset:20480\n\t" \
  "v_mfma_f32_16x16x32_bf16 v[160:163], v[96:99], v[48:51], v[160:163]\n\t" \
  "s_cmp_eq_u32 %[sc], 2\n\t" \
  "s_cselect_b32 %[sc], -1, %[sc]\n\t" \
  "v_mfma_f32_16x16x32_bf16 v[160:163], v[100:103], v[52:55], v[160:163]\n\t" \
  "ds_read_b128 v[64:67], v4 offset:16384\n\t" \
  "v_mfma_f32_16x16x32_bf16 v[176:179], v[100:103], v[60:63], v[176:179]\n\t" \
  "s_cselect_b32 %[sd], %[sneg], 0x8000\n\t" \
  "v_mfma_f32_16x16x32_bf16 v[176:179], v[96:99], v[56:59], v[176:179]\n\t" \
  "ds_read_b128 v[68:71], v5 offset:16384\n\t" \
  "v_mfma_f32_16x16x32_bf16 v[180:183], v[104:107], v[56:59], v[180:183]\n\t" \
  "v_mfma_f32_16x16x32_bf16 v[180:183], v[108:111], v[60:63], v[180:183]\n\t" \
  "ds_read_b128 v[72:75], v4 offset:20480\n\t" \
  "v_mfma_f32_16x16x32_bf16 v[164:167], v[108:111], v[52:55], v[164:167]\n\t" \
  "v_mfma_f32_16x16x32_bf16 v[164:167], v[104:107], v[48:51], v[164:167]\n\t" \
  "ds_read_b128 v[76:79], v5 offset:20480\n\t" \
  "v_mfma_f32_16x16x32_bf16 v[148:151], v[104:107], v[40:43], v[148:151]\n\t" \
  "v_mfma_f32_16x16x32_bf16 v[148:151], v[108:111], v[44:47], v[148:151]\n\t" \
  "ds_read_b128 v[80:83], v4 offset:24576\n\t" \
  "v_mfma_f32_16x16x32_bf16 v[132:135], v[108:111], v[36:39], v[132:135]\n\t" \
  "ds_read_b128 v[84:87], v5 offset:24576\n\t" \
  "v_mfma_f32_16x16x32_bf16 v[132:135], v[104:107], v[32:35], v[132:135]\n\t" \
  "s_add_u32 %[sc], %[sc], 1\n\t" \
  "s_waitcnt lgkmcnt(10)\n\t" \
  "v_mfma_f32_16x16x32_bf16 v[136:139], v[112:115], v[32:35], v[136:139]\n\t" \
  "v_mfma_f32_16x16x32_bf16 v[136:139], v[116:119], v[36:39], v[136:139]\n\t" \
  "ds_read_b128 v[88:91], v4 offset:28672\n\t" \
  "v_mfma_f32_16x16x32_bf16 v[152:155], v[116:119], v[44:47], v[152:155]\n\t" \
  "v_mfma_f32_16x16x32_bf16 v[152:155], v[112:115], v[40:43], v[152:155]\n\t" \
  "ds_read_b128 v[92:95], v5 offset:28672\n\t" \
  "v_mfma_f32_16x16x32_bf16 v[168:171], v[112:115], v[48:51], v[168:171]\n\t" \
  "v_mfma_f32_16x16x32_bf16 v[168:171], v[116:119], v[52:55], v[168:171]\n\t" \
  "v_add_u32_e64 v8, %[sd], v8\n\t" \
  "v_mfma_f32_16x16x32_bf16 v[184:187], v[116:119], v[60:63], v[184:187]\n\t" \
  "v_mfma_f32_16x16x32_bf16 v[184:187], v[112:115], v[56:59], v[184:187]\n\t" \
  "v_add_u32_e32 v9, %[sd], v9\n\t" \
  "s_waitcnt lgkmcnt(8)\n\t" \
  "v_mfma_f32_16x16x32_bf16 v[188:191], v[120:123], v[56:59], v[188:191]\n\t" \
  "v_mfma_f32_16x16x32_bf16 v[188:191], v[124:127], v[60:63], v[188:191]\n\t" \
  "v_add_u32_e64 v10, %[sd], v10\n\t" \
  "v_mfma_f32_16x16x32_bf16 v[172:175], v[124:127], v[52:55], v[172:175]\n\t" \
  "v_mfma_f32_16x16x32_bf16 v[172:175], v[120:123], v[48:51], v[172:175]\n\t" \
  "v_add_u32_e64 v11, %[sd], v11\n\t" \
  "v_mfma_f32_16x16x32_bf16 v[156:159], v[120:123], v[40:43], v[156:159]\n\t" \
  "v_mfma_f32_16x16x32_bf16 v[156:159], v[124:127], v[44:47], v[156:159]\n\t" \
  "v_xor_b32 v4, 0x8000, v4\n\t" \
  "v_mfma_f32_16x16x32_bf16 v[140:143], v[124:127], v[36:39], v[140:143]\n\t" \
  "v_xor_b32 v5, 0x8000, v5\n\t" \
  "v_mfma_f32_16x16x32_bf16 v[140:143], v[120:123], v[32:35], v[140:143]\n\t" \
  "s_waitcnt lgkmcnt(0)\n\t" \
  "s_barrier\n\t" \
  "v_mfma_f32_16x16x32_bf16 v[192:195], v[96:99], v[64:67], v[192:195]\n\t" \
  "ds_read_b128 v[32:35], v6 offset:0\n\t" \
  "v_mfma_f32_16x16x32_bf16 v[192:195], v[100:103], v[68:71], v[192:195]\n\t" \
  "ds_read_b128 v[36:39], v7 offset:0\n\t" \
  "v_mfma_f32_16x16x32_bf16 v[208:211], v[100:103], v[76:79], v[208:211]\n\t" \
  "ds_read_b128 v[40:43], v6 offset:4096\n\t" \
  "v_mfma_f32_16x16x32_bf16 v[208:211], v[96:99], v[72:75], v[208:211]\n\t" \
  "v_mfma_f32_16x16x32_bf16 v[224:227], v[96:99], v[80:83], v[224:227]\n\t" \
  "ds_read_b128 v[44:47], v7 offset:4096\n\t" \
  "v_mfma_f32_16x16x32_bf16 v[224:227], v[100:103], v[84:87], v[224:227]\n\t" \
  "ds_read_b128 v[48:51], v6 offset:8192\n\t" \
  "v_mfma_f32_16x16x32_bf16 v[240:243], v[100:103], v[92:95], v[240:243]\n\t" \
  "ds_read_b128 v[52:55], v7 offset:8192\n\t" \
  "v_mfma_f32_16x16x32_bf16 v[240:243], v[96:99], v[88:91], v[240:243]\n\t" \
  "v_mfma_f32_16x16x32_bf16 v[244:247], v[104:107], v[88:91], v[244:247]\n\t" \
  "ds_read_b64_tr_b16 v[96:97], v8 offset:0\n\t" \
  "v_mfma_f32_16x16x32_bf16 v[244:247], v[108:111], v[92:95], v[244:247]\n\t" \
  "ds_read_b64_tr_b16 v[98:99], v8 offset:4096\n\t" \
  "v_mfma_f32_16x16x32_bf16 v[228:231], v[108:111], v[84:87], v[228:231]\n\t" \
  "ds_read_b64_tr_b16 v[100:101], v8 offset:16384\n\t" \
  "v_mfma_f32_16x16x32_bf16 v[228:231], v[104:107], v[80:83], v[228:231]\n\t" \
  "ds_read_b64_tr_b16 v[102:103], v8 offset:20480\n\t" \
  "v_mfma_f32_16x16x32_bf16 v[212:215], v[104:107], v[72:75], v[212:215]\n\t" \
  "v_mfma_f32_16x16x32_bf16 v[212:215], v[108:111], v[76:79], v[212:215]\n\t" \
  "ds_read_b128 v[56:59], v6 offset:12288\n\t" \
  "v_mfma_f32_16x16x32_bf16 v[196:199], v[108:111], v[68:71], v[196:199]\n\t" \
  "ds_read_b128 v[60:63], v7 offset:12288\n\t" \
  "v_mfma_f32_16x16x32_bf16 v[196:199], v[104:107], v[64:67], v[196:199]\n\t" \
  "v_mfma_f32_16x16x32_bf16 v[200:203], v[112:115], v[64:67], v[200:203]\n\t" \
  "ds_read_b64_tr_b16 v[104:105], v9 offset:0\n\t" \
  "v_mfma_f32_16x16x32_bf16 v[200:203], v[116:119], v[68:71], v[200:203]\n\t" \
  "ds_read_b64_tr_b16 v[106:107], v9 offset:4096\n\t" \
  "v_mfma_f32_16x16x32_bf16 v[216:219], v[116:119], v[76:79], v[216:219]\n\t" \
  "ds_read_b64_tr_b16 v[108:109], v9 offset:16384\n\t" \
  "v_mfma_f32_16x16x32_bf16 v[216:219], v[112:115], v[72:75], v[216:219]\n\t" \
  "ds_read_b64_tr_b16 v[110:111], v9 offset:20480\n\t" \
  "v_mfma_f32_16x16x32_bf16 v[232:235], v[112:115], v[80:83], v[232:235]\n\t" \
  "v_mfma_f32_16x16x32_bf16 v[232:235], v[116:119], v[84:87], v[232:235]\n\t" \
  "v_mfma_f32_16x16x32_bf16 v[248:251], v[116:119], v[92:95], v[248:251]\n\t" \
  "v_mfma_f32_16x16x32_bf16 v[248:251], v[112:115], v[88:91], v[248:251]\n\t" \
  "v_mfma_f32_16x16x32_bf16 v[252:255], v[120:123], v[88:91], v[252:255]\n\t" \
  "ds_read_b64_tr_b16 v[112:113], v10 offset:0\n\t" \
  "v_mfma_f32_16x16x32_bf16 v[252:255], v[124:127], v[92:95], v[252:255]\n\t" \
  "ds_read_b64_tr_b16 v[114:115], v10 offset:4096\n\t" \
  "v_mfma_f32_16x16x32_bf16 v[236:239], v[124:127], v[84:87], v[236:239]\n\t" \
  "ds_read_b64_tr_b16 v[116:117], v10 offset:16384\n\t" \
  "v_mfma_f32_16x16x32_bf16 v[236:239], v[120:123], v[80:83], v[236:239]\n\t" \
  "ds_read_b64_tr_b16 v[118:119], v10 offset:20480\n\t" \
  "v_mfma_f32_16x16x32_bf16 v[220:223], v[120:123], v[72:75], v[220:223]\n\t" \
  "v_mfma_f32_16x16x32_bf16 v[220:223], v[124:127], v[76:79], v[220:223]\n\t" \
  "v_mfma_f32_16x16x32_bf16 v[204:207], v[124:127], v[68:71], v[204:207]\n\t" \
  "v_mfma_f32_16x16x32_bf16 v[204:207], v[120:123], v[64:67], v[204:207]\n\t" \
  "s_waitcnt lgkmcnt(4)\n\t" \
  "s_sub_u32 %[it], %[it], 1\n\t" \
  "v_mfma_f32_16x16x32_bf16 v[128:131], v[96:99], v[32:35], v[128:131]\n\t" \
  "ds_read_b64_tr_b16 v[120:121], v11 offset:0\n\t" \
  "v_mfma_f32_16x16x32_bf16 v[128:131], v[100:103], v[36:39], v[128:131]\n\t" \
  "ds_read_b64_tr_b16 v[122:123], v11 offset:4096\n\t" \
  "v_mfma_f32_16x16x32_bf16 v[144:147], v[100:103], v[44:47], v[144:147]\n\t" \
  "ds_read_b64_tr_b16 v[124:125], v11 offset:16384\n\t" \
  "v_mfma_f32_16x16x32_bf16 v[144:147], v[96:99], v[40:43], v[144:147]\n\t" \
  "ds_read_b64_tr_b16 v[126:127], v11 offset:20480\n\t" \
  "v_mfma_f32_16x16x32_bf16 v[160:163], v[96:99], v[48:51], v[160:163]\n\t" \
  "s_cmp_eq_u32 %[sc], 2\n\t" \
  "s_cselect_b32 %[sc], -1, %[sc]\n\t" \
  "v_mfma_f32_16x16x32_bf16 v[160:163], v[100:103], v[52:55], v[160:163]\n\t" \
  "ds_read_b128 v[64:67], v6 offset:16384\n\t" \
  "v_mfma_f32_16x16x32_bf16 v[176:179], v[100:103], v[60:63], v[176:179]\n\t" \
  "s_cselect_b32 %[sd], %[sneg], 0x8000\n\t" \
  "v_mfma_f32_16x16x32_bf16 v[176:179], v[96:99], v[56:59], v[176:179]\n\t" \
  "ds_read_b128 v[68:71], v7 offset:16384\n\t" \
  "v_mfma_f32_16x16x32_bf16 v[180:183], v[104:107], v[56:59], v[180:183]\n\t" \
  "v_mfma_f32_16x16x32_bf16 v[180:183], v[108:111], v[60:63], v[180:183]\n\t" \
  "ds_read_b128 v[72:75], v6 offset:20480\n\t" \
  "v_mfma_f32_16x16x32_bf16 v[164:167], v[108:111], v[52:55], v[164:167]\n\t" \
  "v_mfma_f32_16x16x32_bf16 v[164:167], v[104:107], v[48:51], v[164:167]\n\t" \
  "ds_read_b128 v[76:79], v7 offset:20480\n\t" \
  "v_mfma_f32_16x16x32_bf16 v[148:151], v[104:107], v[40:43], v[148:151]\n\t" \
  "v_mfma_f32_16x16x32_bf16 v[148:151], v[108:111], v[44:47], v[148:151]\n\t" \
  "ds_read_b128 v[80:83], v6 offset:24576\n\t" \
  "v_mfma_f32_16x16x32_bf16 v[132:135], v[108:111], v[36:39], v[132:135]\n\t" \
  "ds_read_b128 v[84:87], v7 offset:24576\n\t" \
  "v_mfma_f32_16x16x32_bf16 v[132:135], v[104:107], v[32:35], v[132:135]\n\t" \
  "s_add_u32 %[sc], %[sc], 1\n\t" \
  "s_waitcnt lgkmcnt(10)\n\t" \
  "v_mfma_f32_16x16x32_bf16 v[136:139], v[112:115], v[32:35], v[136:139]\n\t" \
  "v_mfma_f32_16x16x32_bf16 v[136:139], v[116:119], v[36:39], v[136:139]\n\t" \
  "ds_read_b128 v[88:91], v6 offset:28672\n\t" \
  "v_mfma_f32_16x16x32_bf16 v[152:155], v[116:119], v[44:47], v[152:155]\n\t" \
  "v_mfma_f32_16x16x32_bf16 v[152:155], v[112:115], v[40:43], v[152:155]\n\t" \
  "ds_read_b128 v[92:95], v7 offset:28672\n\t" \
  "v_mfma_f32_16x16x32_bf16 v[168:171], v[112:115], v[48:51], v[168:171]\n\t" \
  "v_mfma_f32_16x16x32_bf16 v[168:171], v[116:119], v[52:55], v[168:171]\n\t" \
  "v_add_u32_e64 v8, %[sd], v8\n\t" \
  "v_mfma_f32_16x16x32_bf16 v[184:187], v[116:119], v[60:63], v[184:187]\n\t" \
  "v_mfma_f32_16x16x32_bf16 v[184:187], v[112:115], v[56:59], v[184:187]\n\t" \
  "v_add_u32_e32 v9, %[sd], v9\n\t" \
  "s_waitcnt lgkmcnt(8)\n\t" \
  "v_mfma_f32_16x16x32_bf16 v[188:191], v[120:123], v[56:59], v[188:191]\n\t" \
  "v_mfma_f32_16x16x32_bf16 v[188:191], v[124:127], v[60:63], v[188:191]\n\t" \
  "v_add_u32_e64 v10, %[sd], v10\n\t" \
  "v_mfma_f32_16x16x32_bf16 v[172:175], v[124:127], v[52:55], v[172:175]\n\t" \
  "v_mfma_f32_16x16x32_bf16 v[172:175], v[120:123], v[48:51], v[172:175]\n\t" \
  "v_add_u32_e64 v11, %[sd], v11\n\t" \
  "v_mfma_f32_16x16x32_bf16 v[156:159], v[120:123], v[40:43], v[156:159]\n\t" \
  "v_mfma_f32_16x16x32_bf16 v[156:159], v[124:127], v[44:47], v[156:159]\n\t" \
  "v_xor_b32 v6, 0x8000, v6\n\t" \
  "v_mfma_f32_16x16x32_bf16 v[140:143], v[124:127], v[36:39], v[140:143]\n\t" \
  "v_xor_b32 v7, 0x8000, v7\n\t" \
  "v_mfma_f32_16x16x32_bf16 v[140:143], v[120:123], v[32:35], v[140:143]\n\t" \
  "s_waitcnt lgkmcnt(0)\n\t" \
  "s_barrier\n\t" \
  "v_mfma_f32_16x16x32_bf16 v[192:195], v[96:99], v[64:67], v[192:195]\n\t" \
  "ds_read_b128 v[32:35], v4 offset:0\n\t" \
  "v_mfma_f32_16x16x32_bf16 v[192:195], v[100:103], v[68:71], v[192:195]\n\t" \
  "ds_read_b128 v[36:39], v5 offset:0\n\t" \
  "v_mfma_f32_16x16x32_bf16 v[208:211], v[100:103], v[76:79], v[208:211]\n\t" \
  "ds_read_b128 v[40:43], v4 offset:4096\n\t" \
  "v_mfma_f32_16x16x32_bf16 v[208:211], v[96:99], v[72:75], v[208:211]\n\t" \
  "v_mfma_f32_16x16x32_bf16 v[224:227], v[96:99], v[80:83], v[224:227]\n\t" \
  "ds_read_b128 v[44:47], v5 offset:4096\n\t" \
  "v_mfma_f32_16x16x32_bf16 v[224:227], v[100:103], v[84:87], v[224:227]\n\t" \
  "ds_read_b128 v[48:51], v4 offset:8192\n\t" \
  "v_mfma_f32_16x16x32_bf16 v[240:243], v[100:103], v[92:95], v[240:243]\n\t" \
  "ds_read_b128 v[52:55], v5 offset:8192\n\t" \
  "v_mfma_f32_16x16x32_bf16 v[240:243], v[96:99], v[88:91], v[240:243]\n\t" \
  "v_mfma_f32_16x16x32_bf16 v[244:247], v[104:107], v[88:91], v[244:247]\n\t" \
  "ds_read_b64_tr_b16 v[96:97], v8 offset:0\n\t" \
  "v_mfma_f32_16x16x32_bf16 v[244:247], v[108:111], v[92:95], v[244:247]\n\t" \
  "ds_read_b64_tr_b16 v[98:99], v8 offset:4096\n\t" \
  "v_mfma_f32_16x16x32_bf16 v[228:231], v[108:111], v[84:87], v[228:231]\n\t" \
  "ds_read_b64_tr_b16 v[100:101], v8 offset:16384\n\t" \
  "v_mfma_f32_16x16x32_bf16 v[228:231], v[104:107], v[80:83], v[228:231]\n\t" \
  "ds_read_b64_tr_b16 v[102:103], v8 offset:20480\n\t" \
  "v_mfma_f32_16x16x32_bf16 v[212:215], v[104:107], v[72:75], v[212:215]\n\t" \
  "v_mfma_f32_16x16x32_bf16 v[212:215], v[108:111], v[76:79], v[212:215]\n\t" \
  "ds_read_b128 v[56:59], v4 offset:12288\n\t" \
  "v_mfma_f32_16x16x32_bf16 v[196:199], v[108:111], v[68:71], v[196:199]\n\t" \
  "ds_read_b128 v[60:63], v5 offset:12288\n\t" \
  "v_mfma_f32_16x16x32_bf16 v[196:199], v[104:107], v[64:67], v[196:199]\n\t" \
  "v_mfma_f32_16x16x32_bf16 v[200:203], v[112:115], v[64:67], v[200:203]\n\t" \
  "ds_read_b64_tr_b16 v[104:105], v9 offset:0\n\t" \
  "v_mfma_f32_16x16x32_bf16 v[200:203], v[116:119], v[68:71], v[200:203]\n\t" \
  "ds_read_b64_tr_b16 v[106:107], v9 offset:4096\n\t" \
  "v_mfma_f32_16x16x32_bf16 v[216:219], v[116:119], v[76:79], v[216:219]\n\t" \
  "ds_read_b64_tr_b16 v[108:109], v9 offset:16384\n\t" \
  "v_mfma_f32_16x16x32_bf16 v[216:219], v[112:115], v[72:75], v[216:219]\n\t" \
  "ds_read_b64_tr_b16 v[110:111], v9 offset:20480\n\t" \
  "v_mfma_f32_16x16x32_bf16 v[232:235], v[112:115], v[80:83], v[232:235]\n\t" \
  "v_mfma_f32_16x16x32_bf16 v[232:235], v[116:119], v[84:87], v[232:235]\n\t" \
  "s_cmpk_lg_u32 %[it], 0\n\t" \
  "s_nop 0\n\t" \
  "v_mfma_f32_16x16x32_bf16 v[248:251], v[116:119], v[92:95], v[248:251]\n\t" \
  "v_mfma_f32_16x16x32_bf16 v[248:251], v[112:115], v[88:91], v[248:251]\n\t" \
  "v_mfma_f32_16x16x32_bf16 v[252:255], v[120:123], v[88:91], v[252:255]\n\t" \
  "ds_read_b64_tr_b16 v[112:113], v10 offset:0\n\t" \
  "v_mfma_f32_16x16x32_bf16 v[252:255], v[124:127], v[92:95], v[252:255]\n\t" \
  "ds_read_b64_tr_b16 v[114:115], v10 offset:4096\n\t" \
  "v_mfma_f32_16x16x32_bf16 v[236:239], v[124:127], v[84:87], v[236:239]\n\t" \
  "ds_read_b64_tr_b16 v[116:117], v10 offset:16384\n\t" \
  "v_mfma_f32_16x16x32_bf16 v[236:239], v[120:123], v[80:83], v[236:239]\n\t" \
  "ds_read_b64_tr_b16 v[118:119], v10 offset:20480\n\t" \
  "v_mfma_f32_16x16x32_bf16 v[220:223], v[120:123], v[72:75], v[220:223]\n\t" \
  "v_mfma_f32_16x16x32_bf16 v[220:223], v[124:127], v[76:79], v[220:223]\n\t" \
  "v_mfma_f32_16x16x32_bf16 v[204:207], v[124:127], v[68:71], v[204:207]\n\t" \
  "v_mfma_f32_16x16x32_bf16 v[204:207], v[120:123], v[64:67], v[204:207]\n\t" \
  "s_waitcnt lgkmcnt(4)\n\t" \
  "s_cbranch_scc1 1b\n\t" \
  "v_mfma_f32_16x16x32_bf16 v[128:131], v[96:99], v[32:35], v[128:131]\n\t" \
  "ds_read_b64_tr_b16 v[120:121], v11 offset:0\n\t" \
  "v_mfma_f32_16x16x32_bf16 v[128:131], v[100:103], v[36:39], v[128:131]\n\t" \
  "ds_read_b64_tr_b16 v[122:123], v11 offset:4096\n\t" \
  "v_mfma_f32_16x16x32_bf16 v[144:147], v[100:103], v[44:47], v[144:147]\n\t" \
  "ds_read_b64_tr_b16 v[124:125], v11 offset:16384\n\t" \
  "v_mfma_f32_16x16x32_bf16 v[144:147], v[96:99], v[40:43], v[144:147]\n\t" \
  "ds_read_b64_tr_b16 v[126:127], v11 offset:20480\n\t" \
  "v_mfma_f32_16x16x32_bf16 v[160:163], v[96:99], v[48:51], v[160:163]\n\t" \
  "s_cmp_eq_u32 %[sc], 2\n\t" \
  "s_cselect_b32 %[sc], -1, %[sc]\n\t" \
  "v_mfma_f32_16x16x32_bf16 v[160:163], v[100:103], v[52:55], v[160:163]\n\t" \
  "ds_read_b128 v[64:67], v4 offset:16384\n\t" \
  "v_mfma_f32_16x16x32_bf16 v[176:179], v[100:103], v[60:63], v[176:179]\n\t" \
  "s_cselect_b32 %[sd], %[sneg], 0x8000\n\t" \
  "v_mfma_f32_16x16x32_bf16 v[176:179], v[96:99], v[56:59], v[176:179]\n\t" \
  "ds_read_b128 v[68:71], v5 offset:16384\n\t" \
  "v_mfma_f32_16x16x32_bf16 v[180:183], v[104:107], v[56:59], v[180:183]\n\t" \
  "v_mfma_f32_16x16x32_bf16 v[180:183], v[108:111], v[60:63], v[180:183]\n\t" \
  "ds_read_b128 v[72:75], v4 offset:20480\n\t" \
  "v_mfma_f32_16x16x32_bf16 v[164:167], v[108:111], v[52:55], v[164:167]\n\t" \
  "v_mfma_f32_16x16x32_bf16 v[164:167], v[104:107], v[48:51], v[164:167]\n\t" \
  "ds_read_b128 v[76:79], v5 offset:20480\n\t" \
  "v_mfma_f32_16x16x32_bf16 v[148:151], v[104:107], v[40:43], v[148:151]\n\t" \
  "v_mfma_f32_16x16x32_bf16 v[148:151], v[108:111], v[44:47], v[148:151]\n\t" \
  "ds_read_b128 v[80:83], v4 offset:24576\n\t" \
  "v_mfma_f32_16x16x32_bf16 v[132:135], v[108:111], v[36:39], v[132:135]\n\t" \
  "ds_read_b128 v[84:87], v5 offset:24576\n\t" \
  "v_mfma_f32_16x16x32_bf16 v[132:135], v[104:107], v[32:35], v[132:135]\n\t" \
  "s_add_u32 %[sc], %[sc], 1\n\t" \
  "s_waitcnt lgkmcnt(10)\n\t" \
  "v_mfma_f32_16x16x32_bf16 v[136:139], v[112:115], v[32:35], v[136:139]\n\t" \
  "v_mfma_f32_16x16x32_bf16 v[136:139], v[116:119], v[36:39], v[136:139]\n\t" \
  "ds_read_b128 v[88:91], v4 offset:28672\n\t" \
  "v_mfma_f32_16x16x32_bf16 v[152:155], v[116:119], v[44:47], v[152:155]\n\t" \
  "v_mfma_f32_16x16x32_bf16 v[152:155], v[112:115], v[40:43], v[152:155]\n\t" \
  "ds_read_b128 v[92:95], v5 offset:28672\n\t" \
  "v_mfma_f32_16x16x32_bf16 v[168:171], v[112:115], v[48:51], v[168:171]\n\t" \
  "v_mfma_f32_16x16x32_bf16 v[168:171], v[116:119], v[52:55], v[168:171]\n\t" \
  "v_add_u32_e64 v8, %[sd], v8\n\t" \
  "v_mfma_f32_16x16x32_bf16 v[184:187], v[116:119], v[60:63], v[184:187]\n\t" \
  "v_mfma_f32_16x16x32_bf16 v[184:187], v[112:115], v[56:59], v[184:187]\n\t" \
  "v_add_u32_e32 v9, %[sd], v9\n\t" \
  "s_waitcnt lgkmcnt(8)\n\t" \
  "v_mfma_f32_16x16x32_bf16 v[188:191], v[120:123], v[56:59], v[188:191]\n\t" \
  "v_mfma_f32_16x16x32_bf16 v[188:191], v[124:127], v[60:63], v[188:191]\n\t" \
  "v_add_u32_e64 v10, %[sd], v10\n\t" \
  "v_mfma_f32_16x16x32_bf16 v[172:175], v[124:127], v[52:55], v[172:175]\n\t" \
  "v_mfma_f32_16x16x32_bf16 v[172:175], v[120:123], v[48:51], v[172:175]\n\t" \
  "v_add_u32_e64 v11, %[sd], v11\n\t" \
  "v_mfma_f32_16x16x32_bf16 v[156:159], v[120:123], v[40:43], v[156:159]\n\t" \
  "v_mfma_f32_16x16x32_bf16 v[156:159], v[124:127], v[44:47], v[156:159]\n\t" \
  "v_mfma_f32_16x16x32_bf16 v[140:143], v[124:127], v[36:39], v[140:143]\n\t" \
  "v_mfma_f32_16x16x32_bf16 v[140:143], v[120:123], v[32:35], v[140:143]\n\t" \
  "s_waitcnt lgkmcnt(0)\n\t" \
  "s_barrier\n\t" \
  "v_mfma_f32_16x16x32_bf16 v[192:195], v[96:99], v[64:67], v[192:195]\n\t" \
  "ds_read_b128 v[32:35], v6 offset:0\n\t" \
  "v_mfma_f32_16x16x32_bf16 v[192:195], v[100:103], v[68:71], v[192:195]\n\t" \
  "ds_read_b128 v[36:39], v7 offset:0\n\t" \
  "v_mfma_f32_16x16x32_bf16 v[208:211], v[100:103], v[76:79], v[208:211]\n\t" \
  "ds_read_b128 v[40:43], v6 offset:4096\n\t" \
  "v_mfma_f32_16x16x32_bf16 v[208:211], v[96:99], v[72:75], v[208:211]\n\t" \
  "v_mfma_f32_16x16x32_bf16 v[224:227], v[96:99], v[80:83], v[224:227]\n\t" \
  "ds_read_b128 v[44:47], v7 offset:4096\n\t" \
  "v_mfma_f32_16x16x32_bf16 v[224:227], v[100:103], v[84:87], v[224:227]\n\t" \
  "ds_read_b128 v[48:51], v6 offset:8192\n\t" \
  "v_mfma_f32_16x16x32_bf16 v[240:243], v[100:103], v[92:95], v[240:243]\n\t" \
  "ds_read_b128 v[52:55], v7 offset:8192\n\t" \
  "v_mfma_f32_16x16x32_bf16 v[240:243], v[96:99], v[88:91], v[240:243]\n\t" \
  "v_mfma_f32_16x16x32_bf16 v[244:247], v[104:107], v[88:91], v[244:247]\n\t" \
  "ds_read_b64_tr_b16 v[96:97], v8 offset:0\n\t" \
  "v_mfma_f32_16x16x32_bf16 v[244:247], v[108:111], v[92:95], v[244:247]\n\t" \
  "ds_read_b64_tr_b16 v[98:99], v8 offset:4096\n\t" \
  "v_mfma_f32_16x16x32_bf16 v[228:231], v[108:111], v[84:87], v[228:231]\n\t" \
  "ds_read_b64_tr_b16 v[100:101], v8 offset:16384\n\t" \
  "v_mfma_f32_16x16x32_bf16 v[228:231], v[104:107], v[80:83], v[228:231]\n\t" \
  "ds_read_b64_tr_b16 v[102:103], v8 offset:20480\n\t" \
  "v_mfma_f32_16x16x32_bf16 v[212:215], v[104:107], v[72:75], v[212:215]\n\t" \
  "v_mfma_f32_16x16x32_bf16 v[212:215], v[108:111], v[76:79], v[212:215]\n\t" \
  "ds_read_b128 v[56:59], v6 offset:12288\n\t" \
  "v_mfma_f32_16x16x32_bf16 v[196:199], v[108:111], v[68:71], v[196:199]\n\t" \
  "ds_read_b128 v[60:63], v7 offset:12288\n\t" \
  "v_mfma_f32_16x16x32_bf16 v[196:199], v[104:107], v[64:67], v[196:199]\n\t" \
  "v_mfma_f32_16x16x32_bf16 v[200:203], v[112:115], v[64:67], v[200:203]\n\t" \
  "ds_read_b64_tr_b16 v[104:105], v9 offset:0\n\t" \
  "v_mfma_f32_16x16x32_bf16 v[200:203], v[116:119], v[68:71], v[200:203]\n\t" \
  "ds_read_b64_tr_b16 v[106:107], v9 offset:4096\n\t" \
  "v_mfma_f32_16x16x32_bf16 v[216:219], v[116:119], v[76:79], v[216:219]\n\t" \
  "ds_read_b64_tr_b16 v[108:109], v9 offset:16384\n\t" \
  "v_mfma_f32_16x16x32_bf16 v[216:219], v[112:115], v[72:75], v[216:219]\n\t" \
  "ds_read_b64_tr_b16 v[110:111], v9 offset:20480\n\t" \
  "v_mfma_f32_16x16x32_bf16 v[232:235], v[112:115], v[80:83], v[232:235]\n\t" \
  "v_mfma_f32_16x16x32_bf16 v[232:235], v[116:119], v[84:87], v[232:235]\n\t" \
  "v_mfma_f32_16x16x32_bf16 v[248:251], v[116:119], v[92:95], v[248:251]\n\t" \
  "v_mfma_f32_16x16x32_bf16 v[248:251], v[112:115], v[88:91], v[248:251]\n\t" \
  "v_mfma_f32_16x16x32_bf16 v[252:255], v[120:123], v[88:91], v[252:255]\n\t" \
  "ds_read_b64_tr_b16 v[112:113], v10 offset:0\n\t" \
  "v_mfma_f32_16x16x32_bf16 v[252:255], v[124:127], v[92:95], v[252:255]\n\t" \
  "ds_read_b64_tr_b16 v[114:115], v10 offset:4096\n\t" \
  "v_mfma_f32_16x16x32_bf16 v[236:239], v[124:127], v[84:87], v[236:239]\n\t" \
  "ds_read_b64_tr_b16 v[116:117], v10 offset:16384\n\t" \
  "v_mfma_f32_16x16x32_bf16 v[236:239], v[120:123], v[80:83], v[236:239]\n\t" \
  "ds_read_b64_tr_b16 v[118:119], v10 offset:20480\n\t" \
  "v_mfma_f32_16x16x32_bf16 v[220:223], v[120:123], v[72:75], v[220:223]\n\t" \
  "v_mfma_f32_16x16x32_bf16 v[220:223], v[124:127], v[76:79], v[220:223]\n\t" \
  "v_mfma_f32_16x16x32_bf16 v[204:207], v[124:127], v[68:71], v[204:207]\n\t" \
  "v_mfma_f32_16x16x32_bf16 v[204:207], v[120:123], v[64:67], v[204:207]\n\t" \
  "s_waitcnt lgkmcnt(4)\n\t" \
  "s_waitcnt vmcnt(0)\n\t" \
  "v_mfma_f32_16x16x32_bf16 v[128:131], v[96:99], v[32:35], v[128:131]\n\t" \
  "ds_read_b64_tr_b16 v[120:121], v11 offset:0\n\t" \
  "v_mfma_f32_16x16x32_bf16 v[128:131], v[100:103], v[36:39], v[128:131]\n\t" \
  "ds_read_b64_tr_b16 v[122:123], v11 offset:4096\n\t" \
  "v_mfma_f32_16x16x32_bf16 v[144:147], v[100:103], v[44:47], v[144:147]\n\t" \
  "ds_read_b64_tr_b16 v[124:125], v11 offset:16384\n\t" \
  "v_mfma_f32_16x16x32_bf16 v[144:147], v[96:99], v[40:43], v[144:147]\n\t" \
  "ds_read_b64_tr_b16 v[126:127], v11 offset:20480\n\t" \
  "v_mfma_f32_16x16x32_bf16 v[160:163], v[96:99], v[48:51], v[160:163]\n\t" \
  "v_lshlrev_b32_e32 v8, 16, v16\n\t" \
  "s_nop 0\n\t" \
  "v_and_b32_e32 v9, 0xffff0000, v16\n\t" \
  "v_mfma_f32_16x16x32_bf16 v[160:163], v[100:103], v[52:55], v[160:163]\n\t" \
  "ds_read_b128 v[64:67], v6 offset:16384\n\t" \
  "v_lshlrev_b32_e32 v10, 16, v17\n\t" \
  "s_nop 0\n\t" \
  "v_mfma_f32_16x16x32_bf16 v[176:179], v[100:103], v[60:63], v[176:179]\n\t" \
  "v_and_b32_e32 v11, 0xffff0000, v17\n\t" \
  "v_lshlrev_b32_e32 v12, 16, v18\n\t" \
  "s_nop 0\n\t" \
  "v_mfma_f32_16x16x32_bf16 v[176:179], v[96:99], v[56:59], v[176:179]\n\t" \
  "ds_read_b128 v[68:71], v7 offset:16384\n\t" \
  "v_and_b32_e32 v13, 0xffff0000, v18\n\t" \
  "v_mfma_f32_16x16x32_bf16 v[180:183], v[104:107], v[56:59], v[180:183]\n\t" \
  "v_lshlrev_b32_e32 v14, 16, v19\n\t" \
  "s_nop 0\n\t" \
  "v_and_b32_e32 v15, 0xffff0000, v19\n\t" \
  "v_mfma_f32_16x16x32_bf16 v[180:183], v[108:111], v[60:63], v[180:183]\n\t" \
  "ds_read_b128 v[72:75], v6 offset:20480\n\t" \
  "v_mov_b32_e32 v24, v20\n\t" \
  "s_nop 0\n\t" \
  "v_mfma_f32_16x16x32_bf16 v[164:167], v[108:111], v[52:55], v[164:167]\n\t" \
  "v_mov_b32_e32 v25, v21\n\t" \
  "v_mov_b32_e32 v26, v22\n\t" \
  "v_mfma_f32_16x16x32_bf16 v[164:167], v[104:107], v[48:51], v[164:167]\n\t" \
  "ds_read_b128 v[76:79], v7 offset:20480\n\t" \
  "v_mov_b32_e32 v27, v23\n\t" \
  "s_nop 0\n\t" \
  "v_mfma_f32_16x16x32_bf16 v[148:151], v[104:107], v[40:43], v[148:151]\n\t" \
  "v_lshlrev_b32_e32 v16, 16, v24\n\t" \
  "s_nop 0\n\t" \
  "v_and_b32_e32 v17, 0xffff0000, v24\n\t" \
  "v_mfma_f32_16x16x32_bf16 v[148:151], v[108:111], v[44:47], v[148:151]\n\t" \
  "ds_read_b128 v[80:83], v6 offset:24576\n\t" \
  "v_lshlrev_b32_e32 v18, 16, v25\n\t" \
  "s_nop 0\n\t" \
  "v_mfma_f32_16x16x32_bf16 v[132:135], v[108:111], v[36:39], v[132:135]\n\t" \
  "ds_read_b128 v[84:87], v7 offset:24576\n\t" \
  "v_and_b32_e32 v19, 0xffff0000, v25\n\t" \
  "v_mfma_f32_16x16x32_bf16 v[132:135], v[104:107], v[32:35], v[132:135]\n\t" \
  "v_lshlrev_b32_e32 v20, 16, v26\n\t" \
  "s_nop 0\n\t" \
  "v_and_b32_e32 v21, 0xffff0000, v26\n\t" \
  "s_waitcnt lgkmcnt(10)\n\t" \
  "s_nop 0\n\t" \
  "v_mfma_f32_16x16x32_bf16 v[136:139], v[112:115], v[32:35], v[136:139]\n\t" \
  "v_lshlrev_b32_e32 v22, 16, v27\n\t" \
  "s_nop 0\n\t" \
  "v_and_b32_e32 v23, 0xffff0000, v27\n\t" \
  "v_mfma_f32_16x16x32_bf16 v[136:139], v[116:119], v[36:39], v[136:139]\n\t" \
  "ds_read_b128 v[88:91], v6 offset:28672\n\t" \
  "v_max_f32_e32 v128, 0, v128\n\t" \
  "s_nop 0\n\t" \
  "v_mfma_f32_16x16x32_bf16 v[152:155], v[116:119], v[44:47], v[152:155]\n\t" \
  "v_max_f32_e32 v129, 0, v129\n\t" \
  "v_max_f32_e32 v130, 0, v130\n\t" \
  "v_mfma_f32_16x16x32_bf16 v[152:155], v[112:115], v[40:43], v[152:155]\n\t" \
  "ds_read_b128 v[92:95], v7 offset:28672\n\t" \
  "v_max_f32_e32 v131, 0, v131\n\t" \
  "s_nop 0\n\t" \
  "v_mfma_f32_16x16x32_bf16 v[168:171], v[112:115], v[48:51], v[168:171]\n\t" \
  "v_mul_f32_e32 v24, v128, v8\n\t" \
  "v_fmac_f32_e32 v24, v129, v9\n\t" \
  "v_mfma_f32_16x16x32_bf16 v[168:171], v[116:119], v[52:55], v[168:171]\n\t" \
  "v_fmac_f32_e32 v24, v130, v10\n\t" \
  "v_fmac_f32_e32 v24, v131, v11\n\t" \
  "v_mfma_f32_16x16x32_bf16 v[184:187], v[116:119], v[60:63], v[184:187]\n\t" \
  "v_max_f32_e32 v144, 0, v144\n\t" \
  "v_max_f32_e32 v145, 0, v145\n\t" \
  "v_mfma_f32_16x16x32_bf16 v[184:187], v[112:115], v[56:59], v[184:187]\n\t" \
  "v_max_f32_e32 v146, 0, v146\n\t" \
  "v_max_f32_e32 v147, 0, v147\n\t" \
  "s_waitcnt lgkmcnt(8)\n\t" \
  "s_nop 0\n\t" \
  "v_mfma_f32_16x16x32_bf16 v[188:191], v[120:123], v[56:59], v[188:191]\n\t" \
  "v_mul_f32_e32 v25, v144, v8\n\t" \
  "v_fmac_f32_e32 v25, v145, v9\n\t" \
  "v_mfma_f32_16x16x32_bf16 v[188:191], v[124:127], v[60:63], v[188:191]\n\t" \
  "v_fmac_f32_e32 v25, v146, v10\n\t" \
  "v_fmac_f32_e32 v25, v147, v11\n\t" \
  "v_mfma_f32_16x16x32_bf16 v[172:175], v[124:127], v[52:55], v[172:175]\n\t" \
  "v_max_f32_e32 v160, 0, v160\n\t" \
  "v_max_f32_e32 v161, 0, v161\n\t" \
  "v_mfma_f32_16x16x32_bf16 v[172:175], v[120:123], v[48:51], v[172:175]\n\t" \
  "v_max_f32_e32 v162, 0, v162\n\t" \
  "v_max_f32_e32 v163, 0, v163\n\t" \
  "v_mfma_f32_16x16x32_bf16 v[156:159], v[120:123], v[40:43], v[156:159]\n\t" \
  "v_mul_f32_e32 v26, v160, v8\n\t" \
  "v_fmac_f32_e32 v26, v161, v9\n\t" \
  "v_mfma_f32_16x16x32_bf16 v[156:159], v[124:127], v[44:47], v[156:159]\n\t" \
  "v_fmac_f32_e32 v26, v162, v10\n\t" \
  "v_fmac_f32_e32 v26, v163, v11\n\t" \
  "v_mfma_f32_16x16x32_bf16 v[140:143], v[124:127], v[36:39], v[140:143]\n\t" \
  "v_max_f32_e32 v176, 0, v176\n\t" \
  "v_max_f32_e32 v177, 0, v177\n\t" \
  "v_mfma_f32_16x16x32_bf16 v[140:143], v[120:123], v[32:35], v[140:143]\n\t" \
  "v_max_f32_e32 v178, 0, v178\n\t" \
  "v_max_f32_e32 v179, 0, v179\n\t" \
  "s_waitcnt lgkmcnt(6)\n\t" \
  "s_nop 0\n\t" \
  "v_mfma_f32_16x16x32_bf16 v[192:195], v[96:99], v[64:67], v[192:195]\n\t" \
  "v_mul_f32_e32 v27, v176, v8\n\t" \
  "v_fmac_f32_e32 v27, v177, v9\n\t" \
  "v_mfma_f32_16x16x32_bf16 v[192:195], v[100:103], v[68:71], v[192:195]\n\t" \
  "v_fmac_f32_e32 v27, v178, v10\n\t" \
  "v_fmac_f32_e32 v27, v179, v11\n\t" \
  "s_waitcnt lgkmcnt(4)\n\t" \
  "s_nop 0\n\t" \
  "v_mfma_f32_16x16x32_bf16 v[208:211], v[100:103], v[76:79], v[208:211]\n\t" \
  "v_max_f32_e32 v180, 0, v180\n\t" \
  "v_max_f32_e32 v181, 0, v181\n\t" \
  "v_mfma_f32_16x16x32_bf16 v[208:211], v[96:99], v[72:75], v[208:211]\n\t" \
  "v_max_f32_e32 v182, 0, v182\n\t" \
  "v_max_f32_e32 v183, 0, v183\n\t" \
  "s_waitcnt lgkmcnt(2)\n\t" \
  "s_nop 0\n\t" \
  "v_mfma_f32_16x16x32_bf16 v[224:227], v[96:99], v[80:83], v[224:227]\n\t" \
  "v_fmac_f32_e32 v27, v180, v12\n\t" \
  "v_fmac_f32_e32 v27, v181, v13\n\t" \
  "v_mfma_f32_16x16x32_bf16 v[224:227], v[100:103], v[84:87], v[224:227]\n\t" \
  "v_fmac_f32_e32 v27, v182, v14\n\t" \
  "v_fmac_f32_e32 v27, v183, v15\n\t" \
  "s_waitcnt lgkmcnt(0)\n\t" \
  "s_nop 0\n\t" \
  "v_mfma_f32_16x16x32_bf16 v[240:243], v[100:103], v[92:95], v[240:243]\n\t" \
  "v_max_f32_e32 v164, 0, v164\n\t" \
  "v_max_f32_e32 v165, 0, v165\n\t" \
  "v_mfma_f32_16x16x32_bf16 v[240:243], v[96:99], v[88:91], v[240:243]\n\t" \
  "v_max_f32_e32 v166, 0, v166\n\t" \
  "v_max_f32_e32 v167, 0, v167\n\t" \
  "v_mfma_f32_16x16x32_bf16 v[244:247], v[104:107], v[88:91], v[244:247]\n\t" \
  "v_fmac_f32_e32 v26, v164, v12\n\t" \
  "v_fmac_f32_e32 v26, v165, v13\n\t" \
  "v_mfma_f32_16x16x32_bf16 v[244:247], v[108:111], v[92:95], v[244:247]\n\t" \
  "v_fmac_f32_e32 v26, v166, v14\n\t" \
  "v_fmac_f32_e32 v26, v167, v15\n\t" \
  "v_mfma_f32_16x16x32_bf16 v[228:231], v[108:111], v[84:87], v[228:231]\n\t" \
  "v_max_f32_e32 v148, 0, v148\n\t" \
  "v_max_f32_e32 v149, 0, v149\n\t" \
  "v_mfma_f32_16x16x32_bf16 v[228:231], v[104:107], v[80:83], v[228:231]\n\t" \
  "v_max_f32_e32 v150, 0, v150\n\t" \
  "v_max_f32_e32 v151, 0, v151\n\t" \
  "v_mfma_f32_16x16x32_bf16 v[212:215], v[104:107], v[72:75], v[212:215]\n\t" \
  "v_fmac_f32_e32 v25, v148, v12\n\t" \
  "v_fmac_f32_e32 v25, v149, v13\n\t" \
  "v_mfma_f32_16x16x32_bf16 v[212:215], v[108:111], v[76:79], v[212:215]\n\t" \
  "v_fmac_f32_e32 v25, v150, v14\n\t" \
  "v_fmac_f32_e32 v25, v151, v15\n\t" \
  "v_mfma_f32_16x16x32_bf16 v[196:199], v[108:111], v[68:71], v[196:199]\n\t" \
  "v_max_f32_e32 v132, 0, v132\n\t" \
  "v_max_f32_e32 v133, 0, v133\n\t" \
  "v_mfma_f32_16x16x32_bf16 v[196:199], v[104:107], v[64:67], v[196:199]\n\t" \
  "v_max_f32_e32 v134, 0, v134\n\t" \
  "v_max_f32_e32 v135, 0, v135\n\t" \
  "v_mfma_f32_16x16x32_bf16 v[200:203], v[112:115], v[64:67], v[200:203]\n\t" \
  "v_fmac_f32_e32 v24, v132, v12\n\t" \
  "v_fmac_f32_e32 v24, v133, v13\n\t" \
  "v_mfma_f32_16x16x32_bf16 v[200:203], v[116:119], v[68:71], v[200:203]\n\t" \
  "v_fmac_f32_e32 v24, v134, v14\n\t" \
  "v_fmac_f32_e32 v24, v135, v15\n\t" \
  "v_mfma_f32_16x16x32_bf16 v[216:219], v[116:119], v[76:79], v[216:219]\n\t" \
  "v_max_f32_e32 v136, 0, v136\n\t" \
  "v_max_f32_e32 v137, 0, v137\n\t" \
  "v_mfma_f32_16x16x32_bf16 v[216:219], v[112:115], v[72:75], v[216:219]\n\t" \
  "v_max_f32_e32 v138, 0, v138\n\t" \
  "v_max_f32_e32 v139, 0, v139\n\t" \
  "v_mfma_f32_16x16x32_bf16 v[232:235], v[112:115], v[80:83], v[232:235]\n\t" \
  "v_fmac_f32_e32 v24, v136, v16\n\t" \
  "v_fmac_f32_e32 v24, v137, v17\n\t" \
  "v_mfma_f32_16x16x32_bf16 v[232:235], v[116:119], v[84:87], v[232:235]\n\t" \
  "v_fmac_f32_e32 v24, v138, v18\n\t" \
  "v_fmac_f32_e32 v24, v139, v19\n\t" \
  "v_mfma_f32_16x16x32_bf16 v[248:251], v[116:119], v[92:95], v[248:251]\n\t" \
  "v_max_f32_e32 v152, 0, v152\n\t" \
  "v_max_f32_e32 v153, 0, v153\n\t" \
  "v_mfma_f32_16x16x32_bf16 v[248:251], v[112:115], v[88:91], v[248:251]\n\t" \
  "v_max_f32_e32 v154, 0, v154\n\t" \
  "v_max_f32_e32 v155, 0, v155\n\t" \
  "v_mfma_f32_16x16x32_bf16 v[252:255], v[120:123], v[88:91], v[252:255]\n\t" \
  "v_fmac_f32_e32 v25, v152, v16\n\t" \
  "v_fmac_f32_e32 v25, v153, v17\n\t" \
  "v_mfma_f32_16x16x32_bf16 v[252:255], v[124:127], v[92:95], v[252:255]\n\t" \
  "v_fmac_f32_e32 v25, v154, v18\n\t" \
  "v_fmac_f32_e32 v25, v155, v19\n\t" \
  "v_mfma_f32_16x16x32_bf16 v[236:239], v[124:127], v[84:87], v[236:239]\n\t" \
  "v_max_f32_e32 v168, 0, v168\n\t" \
  "v_max_f32_e32 v169, 0, v169\n\t" \
  "v_mfma_f32_16x16x32_bf16 v[236:239], v[120:123], v[80:83], v[236:239]\n\t" \
  "v_max_f32_e32 v170, 0, v170\n\t" \
  "v_max_f32_e32 v171, 0, v171\n\t" \
  "v_mfma_f32_16x16x32_bf16 v[220:223], v[120:123], v[72:75], v[220:223]\n\t" \
  "v_fmac_f32_e32 v26, v168, v16\n\t" \
  "v_fmac_f32_e32 v26, v169, v17\n\t" \
  "v_mfma_f32_16x16x32_bf16 v[220:223], v[124:127], v[76:79], v[220:223]\n\t" \
  "v_fmac_f32_e32 v26, v170, v18\n\t" \
  "v_fmac_f32_e32 v26, v171, v19\n\t" \
  "v_mfma_f32_16x16x32_bf16 v[204:207], v[124:127], v[68:71], v[204:207]\n\t" \
  "v_max_f32_e32 v184, 0, v184\n\t" \
  "v_max_f32_e32 v185, 0, v185\n\t" \
  "v_mfma_f32_16x16x32_bf16 v[204:207], v[120:123], v[64:67], v[204:207]\n\t" \
  "v_max_f32_e32 v186, 0, v186\n\t" \
  "v_max_f32_e32 v187, 0, v187\n\t" \
  "s_nop 7\n\t" \
  "v_fmac_f32_e32 v27, v184, v16\n\t" \
  "v_fmac_f32_e32 v27, v185, v17\n\t" \
  "v_fmac_f32_e32 v27, v186, v18\n\t" \
  "v_fmac_f32_e32 v27, v187, v19\n\t" \
  "v_max_f32_e32 v188, 0, v188\n\t" \
  "v_max_f32_e32 v189, 0, v189\n\t" \
  "v_max_f32_e32 v190, 0, v190\n\t" \
  "v_max_f32_e32 v191, 0, v191\n\t" \
  "v_fmac_f32_e32 v27, v188, v20\n\t" \
  "v_fmac_f32_e32 v27, v189, v21\n\t" \
  "v_fmac_f32_e32 v27, v190, v22\n\t" \
  "v_fmac_f32_e32 v27, v191, v23\n\t" \
  "v_max_f32_e32 v172, 0, v172\n\t" \
  "v_max_f32_e32 v173, 0, v173\n\t" \
  "v_max_f32_e32 v174, 0, v174\n\t" \
  "v_max_f32_e32 v175, 0, v175\n\t" \
  "v_fmac_f32_e32 v26, v172, v20\n\t" \
  "v_fmac_f32_e32 v26, v173, v21\n\t" \
  "v_fmac_f32_e32 v26, v174, v22\n\t" \
  "v_fmac_f32_e32 v26, v175, v23\n\t" \
  "v_max_f32_e32 v156, 0, v156\n\t" \
  "v_max_f32_e32 v157, 0, v157\n\t" \
  "v_max_f32_e32 v158, 0, v158\n\t" \
  "v_max_f32_e32 v159, 0, v159\n\t" \
  "v_fmac_f32_e32 v25, v156, v20\n\t" \
  "v_fmac_f32_e32 v25, v157, v21\n\t" \
  "v_fmac_f32_e32 v25, v158, v22\n\t" \
  "v_fmac_f32_e32 v25, v159, v23\n\t" \
  "v_max_f32_e32 v140, 0, v140\n\t" \
  "v_max_f32_e32 v141, 0, v141\n\t" \
  "v_max_f32_e32 v142, 0, v142\n\t" \
  "v_max_f32_e32 v143, 0, v143\n\t" \
  "v_fmac_f32_e32 v24, v140, v20\n\t" \
  "v_fmac_f32_e32 v24, v141, v21\n\t" \
  "v_fmac_f32_e32 v24, v142, v22\n\t" \
  "v_fmac_f32_e32 v24, v143, v23\n\t" \
  "v_max_f32_e32 v192, 0, v192\n\t" \
  "v_max_f32_e32 v193, 0, v193\n\t" \
  "v_max_f32_e32 v194, 0, v194\n\t" \
  "v_max_f32_e32 v195, 0, v195\n\t" \
  "v_mul_f32_e32 v28, v192, v8\n\t" \
  "v_fmac_f32_e32 v28, v193, v9\n\t" \
  "v_fmac_f32_e32 v28, v194, v10\n\t" \
  "v_fmac_f32_e32 v28, v195, v11\n\t" \
  "v_max_f32_e32 v208, 0, v208\n\t" \
  "v_max_f32_e32 v209, 0, v209\n\t" \
  "v_max_f32_e32 v210, 0, v210\n\t" \
  "v_max_f32_e32 v211, 0, v211\n\t" \
  "v_mul_f32_e32 v29, v208, v8\n\t" \
  "v_fmac_f32_e32 v29, v209, v9\n\t" \
  "v_fmac_f32_e32 v29, v210, v10\n\t" \
  "v_fmac_f32_e32 v29, v211, v11\n\t" \
  "v_max_f32_e32 v224, 0, v224\n\t" \
  "v_max_f32_e32 v225, 0, v225\n\t" \
  "v_max_f32_e32 v226, 0, v226\n\t" \
  "v_max_f32_e32 v227, 0, v227\n\t" \
  "v_mul_f32_e32 v30, v224, v8\n\t" \
  "v_fmac_f32_e32 v30, v225, v9\n\t" \
  "v_fmac_f32_e32 v30, v226, v10\n\t" \
  "v_fmac_f32_e32 v30, v227, v11\n\t" \
  "v_max_f32_e32 v240, 0, v240\n\t" \
  "v_max_f32_e32 v241, 0, v241\n\t" \
  "v_max_f32_e32 v242, 0, v242\n\t" \
  "v_max_f32_e32 v243, 0, v243\n\t" \
  "v_mul_f32_e32 v31, v240, v8\n\t" \
  "v_fmac_f32_e32 v31, v241, v9\n\t" \
  "v_fmac_f32_e32 v31, v242, v10\n\t" \
  "v_fmac_f32_e32 v31, v243, v11\n\t" \
  "v_max_f32_e32 v244, 0, v244\n\t" \
  "v_max_f32_e32 v245, 0, v245\n\t" \
  "v_max_f32_e32 v246, 0, v246\n\t" \
  "v_max_f32_e32 v247, 0, v247\n\t" \
  "v_fmac_f32_e32 v31, v244, v12\n\t" \
  "v_fmac_f32_e32 v31, v245, v13\n\t" \
  "v_fmac_f32_e32 v31, v246, v14\n\t" \
  "v_fmac_f32_e32 v31, v247, v15\n\t" \
  "v_max_f32_e32 v228, 0, v228\n\t" \
  "v_max_f32_e32 v229, 0, v229\n\t" \
  "v_max_f32_e32 v230, 0, v230\n\t" \
  "v_max_f32_e32 v231, 0, v231\n\t" \
  "v_fmac_f32_e32 v30, v228, v12\n\t" \
  "v_fmac_f32_e32 v30, v229, v13\n\t" \
  "v_fmac_f32_e32 v30, v230, v14\n\t" \
  "v_fmac_f32_e32 v30, v231, v15\n\t" \
  "v_max_f32_e32 v212, 0, v212\n\t" \
  "v_max_f32_e32 v213, 0, v213\n\t" \
  "v_max_f32_e32 v214, 0, v214\n\t" \
  "v_max_f32_e32 v215, 0, v215\n\t" \
  "v_fmac_f32_e32 v29, v212, v12\n\t" \
  "v_fmac_f32_e32 v29, v213, v13\n\t" \
  "v_fmac_f32_e32 v29, v214, v14\n\t" \
  "v_fmac_f32_e32 v29, v215, v15\n\t" \
  "v_max_f32_e32 v196, 0, v196\n\t" \
  "v_max_f32_e32 v197, 0, v197\n\t" \
  "v_max_f32_e32 v198, 0, v198\n\t" \
  "v_max_f32_e32 v199, 0, v199\n\t" \
  "v_fmac_f32_e32 v28, v196, v12\n\t" \
  "v_fmac_f32_e32 v28, v197, v13\n\t" \
  "v_fmac_f32_e32 v28, v198, v14\n\t" \
  "v_fmac_f32_e32 v28, v199, v15\n\t" \
  "v_max_f32_e32 v200, 0, v200\n\t" \
  "v_max_f32_e32 v201, 0, v201\n\t" \
  "v_max_f32_e32 v202, 0, v202\n\t" \
  "v_max_f32_e32 v203, 0, v203\n\t" \
  "v_fmac_f32_e32 v28, v200, v16\n\t" \
  "v_fmac_f32_e32 v28, v201, v17\n\t" \
  "v_fmac_f32_e32 v28, v202, v18\n\t" \
  "v_fmac_f32_e32 v28, v203, v19\n\t" \
  "v_max_f32_e32 v216, 0, v216\n\t" \
  "v_max_f32_e32 v217, 0, v217\n\t" \
  "v_max_f32_e32 v218, 0, v218\n\t" \
  "v_max_f32_e32 v219, 0, v219\n\t" \
  "v_fmac_f32_e32 v29, v216, v16\n\t" \
  "v_fmac_f32_e32 v29, v217, v17\n\t" \
  "v_fmac_f32_e32 v29, v218, v18\n\t" \
  "v_fmac_f32_e32 v29, v219, v19\n\t" \
  "v_max_f32_e32 v232, 0, v232\n\t" \
  "v_max_f32_e32 v233, 0, v233\n\t" \
  "v_max_f32_e32 v234, 0, v234\n\t" \
  "v_max_f32_e32 v235, 0, v235\n\t" \
  "v_fmac_f32_e32 v30, v232, v16\n\t" \
  "v_fmac_f32_e32 v30, v233, v17\n\t" \
  "v_fmac_f32_e32 v30, v234, v18\n\t" \
  "v_fmac_f32_e32 v30, v235, v19\n\t" \
  "v_max_f32_e32 v248, 0, v248\n\t" \
  "v_max_f32_e32 v249, 0, v249\n\t" \
  "v_max_f32_e32 v250, 0, v250\n\t" \
  "v_max_f32_e32 v251, 0, v251\n\t" \
  "v_fmac_f32_e32 v31, v248, v16\n\t" \
  "v_fmac_f32_e32 v31, v249, v17\n\t" \
  "v_fmac_f32_e32 v31, v250, v18\n\t" \
  "v_fmac_f32_e32 v31, v251, v19\n\t" \
  "v_max_f32_e32 v252, 0, v252\n\t" \
  "v_max_f32_e32 v253, 0, v253\n\t" \
  "v_max_f32_e32 v254, 0, v254\n\t" \
  "v_max_f32_e32 v255, 0, v255\n\t" \
  "v_fmac_f32_e32 v31, v252, v20\n\t" \
  "v_fmac_f32_e32 v31, v253, v21\n\t" \
  "v_fmac_f32_e32 v31, v254, v22\n\t" \
  "v_fmac_f32_e32 v31, v255, v23\n\t" \
  "v_max_f32_e32 v236, 0, v236\n\t" \
  "v_max_f32_e32 v237, 0, v237\n\t" \
  "v_max_f32_e32 v238, 0, v238\n\t" \
  "v_max_f32_e32 v239, 0, v239\n\t" \
  "v_fmac_f32_e32 v30, v236, v20\n\t" \
  "v_fmac_f32_e32 v30, v237, v21\n\t" \
  "v_fmac_f32_e32 v30, v238, v22\n\t" \
  "v_fmac_f32_e32 v30, v239, v23\n\t" \
  "v_max_f32_e32 v220, 0, v220\n\t" \
  "v_max_f32_e32 v221, 0, v221\n\t" \
  "v_max_f32_e32 v222, 0, v222\n\t" \
  "v_max_f32_e32 v223, 0, v223\n\t" \
  "v_fmac_f32_e32 v29, v220, v20\n\t" \
  "v_fmac_f32_e32 v29, v221, v21\n\t" \
  "v_fmac_f32_e32 v29, v222, v22\n\t" \
  "v_fmac_f32_e32 v29, v223, v23\n\t" \
  "v_max_f32_e32 v204, 0, v204\n\t" \
  "v_max_f32_e32 v205, 0, v205\n\t" \
  "v_max_f32_e32 v206, 0, v206\n\t" \
  "v_max_f32_e32 v207, 0, v207\n\t" \
  "v_fmac_f32_e32 v28, v204, v20\n\t" \
  "v_fmac_f32_e32 v28, v205, v21\n\t" \
  "v_fmac_f32_e32 v28, v206, v22\n\t" \
  "v_fmac_f32_e32 v28, v207, v23\n\t" \
  "s_nop 2\n\t" \
  "v_permlane32_swap_b32 v24, v25\n\t" \
  "v_permlane32_swap_b32 v26, v27\n\t" \
  "v_permlane32_swap_b32 v28, v29\n\t" \
  "v_permlane32_swap_b32 v30, v31\n\t" \
  "v_add_f32 v24, v24, v25\n\t" \
  "v_add_f32 v26, v26, v27\n\t" \
  "v_add_f32 v28, v28, v29\n\t" \
  "v_add_f32 v30, v30, v31\n\t" \
  "s_nop 1\n\t" \
  "v_permlane16_swap_b32 v24, v26\n\t" \
  "v_permlane16_swap_b32 v28, v30\n\t" \
  "v_add_f32 %[a0], v24, v26\n\t" \
  "v_add_f32 %[b0], v28, v30\n\t"

__global__ __launch_bounds__(512, 2) void wd_main(const int* idx_, const char* table_, const char* W1_, const unsigned short* b1_, const unsigned short* W2_,
                                                  const unsigned short* xw_, const unsigned short* ww_, const unsigned short* bw_, const unsigned short* b2_,
                                                  __hip_bfloat16* out_, unsigned long long* ws_, int V_, int pad_) {
  extern __shared__ __attribute__((aligned(16))) char lds[];
  u64x8 ka; u64x4 kb;
  asm volatile("s_load_dwordx16 %0, %2, 0x0\n\ts_load_dwordx8 %1, %2, 0x40" : "=&s"(ka), "=&s"(kb) : "s"(__builtin_amdgcn_kernarg_segment_ptr()) : "memory");
  __builtin_amdgcn_sched_barrier(0);
  const int wid = __builtin_amdgcn_readfirstlane(threadIdx.x >> 6);
#define LANE(x) int x; asm volatile("v_mbcnt_lo_u32_b32 %0, -1, 0\n\tv_mbcnt_hi_u32_b32 %0, -1, %0" : "=v"(x)); __builtin_assume(x >= 0 && x < 64)
  const int bid = blockIdx.x, xcd = bid & 7, j = bid >> 3, mt = xcd * 16 + (j >> 1), nh = j & 1;
  const unsigned ldsbase = (unsigned)(size_t)(LAS char*)lds;
  LAS float* red = (LAS float*)lds;
  unsigned ia0, ia1, ia2, ia3;
  {
    const int w = wid & 3;
    const unsigned c0 = (unsigned)((mt * 128 + nh * 64 + 16 * w) * (F_ * 4)) + 4096u, c1 = (unsigned)((mt * 128 + (1 - nh) * 64 + 16 * w) * (F_ * 4)) + 4096u;
    const unsigned o0 = (threadIdx.x & 63) * 16u, o1 = min(1024u + o0, 1648u);
    ia0 = c0 + o0; ia1 = c0 + o1 - 1024u; ia2 = c1 + o0 - 2048u; ia3 = c1 + o1 - 3072u;
  }
  asm volatile("s_waitcnt lgkmcnt(0)" : "+s"(ka), "+s"(kb), "+v"(ia0), "+v"(ia1), "+v"(ia2), "+v"(ia3)::"memory");
  const char* idx = (const char*)ka[0]; const char* table = (const char*)ka[1]; const char* W1 = (const char*)ka[2];
  const char* b1 = (const char*)ka[3]; const char* W2 = (const char*)ka[4];
  const GAS unsigned short *xw = (const GAS unsigned short*)ka[5], *ww = (const GAS unsigned short*)ka[6], *bw = (const GAS unsigned short*)ka[7], *b2 = (const GAS unsigned short*)kb[0];
  GAS unsigned short* out = (GAS unsigned short*)kb[1]; gu64* ws = (gu64*)kb[2];
  const int V = (int)(unsigned)kb[3];

  asm volatile("s_load_dwordx16 s[64:79], %0, 0x0\n\ts_load_dwordx16 s[80:95], %0, 0x40" ::"s"(b1 + (nh * 256 + (wid & 3) * 64) * 2) : "memory", SCLOB);
  __builtin_amdgcn_sched_barrier(0);
  if (__builtin_expect(wid < 4, 0)) {
    const int c = wid;
    LANE(lane);
    const int fr = lane & 15, fq = lane >> 4;
    __builtin_amdgcn_s_setprio(2);
    ia0 = ldsbase + (unsigned)(fr * 256 + ((fq ^ fr) * 16));
    { const int q = (lane & 15) >> 2, p = lane & 3, k = 8 * fq + q, fk = (k & 3) | (((k >> 3) & 1) << 2);
      ia1 = ldsbase + (unsigned)(B_BASE + (fq >> 1) * 8192 + q * 1024 + (fq & 1) * 512 + (((4 * c) ^ fk) * 32) + p * 8); }
    unsigned vo = (unsigned)(nh * 256 + c * 64 + fq * 4) * 2u;
    int it = F_ - 1; unsigned sc = 0, sd;
    asm volatile(CONSUMER_ASM
                 : [a0] "+v"(ia0), [b0] "+v"(ia1), [vo] "+v"(vo), [it] "+s"(it), [sc] "+s"(sc), [sd] "=&s"(sd)
                 : [pw2] "s"(W2), [sneg] "s"(-2 * B_SLOT)
                 : "memory", "scc", "v4", "v5", "v6", "v7", "v8", "v9", "v10", "v11", "v12", "v13", "v14", "v15", "v16", "v17", "v18", "v19", "v20", "v21", "v22", "v23", "v24", "v25", "v26", "v27", "v28", "v29", "v30", "v31", "v32", "v33", "v34", "v35", "v36", "v37", "v38", "v39", "v40", "v41", "v42", "v43", "v44", "v45", "v46", "v47", "v48", "v49", "v50", "v51", "v52", "v53", "v54", "v55", "v56", "v57", "v58", "v59", "v60", "v61", "v62", "v63", "v64", "v65", "v66", "v67", "v68", "v69", "v70", "v71", "v72", "v73", "v74", "v75", "v76", "v77", "v78", "v79", "v80", "v81", "v82", "v83", "v84", "v85", "v86", "v87", "v88", "v89", "v90", "v91", "v92", "v93", "v94", "v95", "v96", "v97", "v98", "v99", "v100", "v101", "v102", "v103", "v104", "v105", "v106", "v107", "v108", "v109", "v110", "v111", "v112", "v113", "v114", "v115", "v116", "v117", "v118", "v119", "v120", "v121", "v122", "v123", "v124", "v125", "v126", "v127", "v128", "v129", "v130", "v131", "v132", "v133", "v134", "v135", "v136", "v137", "v138", "v139", "v140", "v141", "v142", "v143", "v144", "v145", "v146", "v147", "v148", "v149", "v150", "v151", "v152", "v153", "v154", "v155", "v156", "v157", "v158", "v159", "v160", "v161", "v162", "v163", "v164", "v165", "v166", "v167", "v168", "v169", "v170", "v171", "v172", "v173", "v174", "v175", "v176", "v177", "v178", "v179", "v180", "v181", "v182", "v183", "v184", "v185", "v186", "v187", "v188", "v189", "v190", "v191", "v192", "v193", "v194", "v195", "v196", "v197", "v198", "v199", "v200", "v201", "v202", "v203", "v204", "v205", "v206", "v207", "v208", "v209", "v210", "v211", "v212", "v213", "v214", "v215", "v216", "v217", "v218", "v219", "v220", "v221", "v222", "v223", "v224", "v225", "v226", "v227", "v228", "v229", "v230", "v231", "v232", "v233", "v234", "v235", "v236", "v237", "v238", "v239", "v240", "v241", "v242", "v243", "v244", "v245", "v246", "v247", "v248", "v249", "v250", "v251", "v252", "v253", "v254", "v255", "s64", "s65", "s66", "s67", "s68", "s69", "s70", "s71", "s72", "s73", "s74", "s75", "s76", "s77", "s78", "s79", "s80", "s81", "s82", "s83", "s84", "s85", "s86", "s87", "s88", "s89", "s90", "s91", "s92", "s93", "s94", "s95");
    LANE(l2);
    { const int fq2 = l2 >> 4, m = ((fq2 & 1) << 1) | (fq2 >> 1);
      red[(16 * m + (l2 & 15)) * 4 + c] = __uint_as_float(ia0); red[(16 * m + 64 + (l2 & 15)) * 4 + c] = __uint_as_float(ia1); }
    asm volatile("s_waitcnt lgkmcnt(0)\n\ts_barrier" ::: "memory");
  } else {
    const int w = wid & 3;
    const unsigned mB = ldsbase + B_BASE + (unsigned)w * 8192u;
    dma4(idx - 4096, mB + 2 * B_SLOT, ia0, ia1, ia2, ia3);
    __builtin_amdgcn_sched_barrier(0);
    LANE(lane);
    const int r4 = lane >> 4, l16 = lane & 15;
    const unsigned toff = (unsigned)((4 * j + w) * 1024) + (((unsigned)lane >> 3) << 7);
    unsigned tdummy = 0;
    auto touch = [&](int f) {
      const char* sb = W1 + (size_t)(f < F_ ? f : F_ - 1) * 131072;
      asm volatile("global_load_dword %0, %1, %2" : "+v"(tdummy) : "v"(toff), "s"(sb) : "memory");
    };
    const unsigned bo0 = (unsigned)((16 * w) * 1024 + nh * 512) + (((unsigned)lane >> 5) << 13) + ((((unsigned)lane & 31u) << 4) ^ (((unsigned)lane >> 5) << 7));
    const unsigned bo1 = bo0 ^ 32u, bo2 = bo0 ^ 64u, bo3 = bo0 ^ 96u;
    auto loadB = [&](int t, unsigned slot) {
      const char* sb = W1 + (size_t)t * 65536;
      dma4(sb, mB + slot, bo0, bo1, bo2, bo3);
      dma4(sb + 4096, mB + slot + 4096, bo0, bo1, bo2, bo3);
    };
    loadB(0, 0); touch(0); touch(1);
    __builtin_amdgcn_sched_barrier(0);
    unsigned ac[4];
#pragma unroll
    for (int i = 0; i < 4; ++i) ac[i] = ((unsigned)((l16 ^ r4) << 4) ^ (unsigned)(64 * i)) + (unsigned)(4096 - i * 1024);
    const char* tb = table - 4096;
    const unsigned mA_lead = ldsbase + (unsigned)(nh * 64 + 16 * w) * 256u, mA_fol = ldsbase + (unsigned)((1 - nh) * 64 + 16 * w) * 256u;
#define LOADA(LEAD, PAR, REG, SLOT) dma4(tb, ((LEAD) ? mA_lead : mA_fol) + (SLOT) * A_SLOT, \
      bcast16<((LEAD) ? 0 : 4) + 8 * (PAR)>(REG) + ac[0], bcast16<((LEAD) ? 1 : 5) + 8 * (PAR)>(REG) + ac[1], \
      bcast16<((LEAD) ? 2 : 6) + 8 * (PAR)>(REG) + ac[2], bcast16<((LEAD) ? 3 : 7) + 8 * (PAR)>(REG) + ac[3])
    auto fix = [&](unsigned r) { int v = (int)r; v = v < 0 ? v + V : v; unsigned u = (unsigned)v < (unsigned)V ? (unsigned)v : 0u; return u * 256u; };
    const int p = l16 & 7, par = l16 >> 3;
    const LAS unsigned* ist = (const LAS unsigned*)(lds + B_BASE + 2 * B_SLOT + w * 8192 + (p >> 2) * 2048 + ((4 * (p & 3) + r4) * F_ + par) * 4);
    unsigned R[13];
    waitv<10>();
    R[0] = fix(ist[0]);
    { const unsigned l0 = bcast16<0>(R[0]) + ac[0], l1 = bcast16<1>(R[0]) + ac[1], l2 = bcast16<2>(R[0]) + ac[2], l3 = bcast16<3>(R[0]) + ac[3];
      const unsigned f0 = bcast16<4>(R[0]) + ac[0], f1 = bcast16<5>(R[0]) + ac[1], f2 = bcast16<6>(R[0]) + ac[2], f3 = bcast16<7>(R[0]) + ac[3];
      dma4h<0>(tb, mA_lead, l0, l1, l2, l3); dma4h<0>(tb, mA_fol, f0, f1, f2, f3);
      dma4h<1>(tb, mA_lead, l0, l1, l2, l3); dma4h<1>(tb, mA_fol, f0, f1, f2, f3);
    }
    loadB(1, B_SLOT);
    LOADA(1, 1, R[0], 1);
    waitv<20>();
    bar();
    LOADA(0, 1, R[0], 1);
#pragma unroll
    for (int f = 2; f < 2 + TDIST; ++f) touch(f);
#pragma unroll
    for (int i = 1; i < 13; ++i) R[i] = fix(ist[2 * i]);
    asm volatile("s_waitcnt lgkmcnt(0)" : "+v"(R[1]), "+v"(R[2]), "+v"(R[3]), "+v"(R[4]), "+v"(R[5]), "+v"(R[6]), "+v"(R[7]), "+v"(R[8]), "+v"(R[9]), "+v"(R[10]), "+v"(R[11]), "+v"(R[12])::"memory");
    waitv<8 + TDIST>();
    bar();
    loadB(2, 2 * B_SLOT);
    loadB(3, 0);
    waitv<8>();
    unsigned bslot = B_SLOT;
#define NEXTSLOT() bslot = bslot == 2 * B_SLOT ? 0 : bslot + B_SLOT
    for (int i = 0; i < 12; ++i) {
      unsigned cur = R[1];
#pragma unroll
      for (int q = 1; q < 12; ++q) R[q] = R[q + 1];
      bar();
      LOADA(1, 0, cur, 0); touch(2 * i + 2 + TDIST);
      loadB(4 * i + 4, bslot); NEXTSLOT();
      waitv<13>();
      bar();
      LOADA(0, 0, cur, 0);
      loadB(4 * i + 5, bslot); NEXTSLOT();
      waitv<8>();
      bar();
      LOADA(1, 1, cur, 1); touch(2 * i + 3 + TDIST);
      loadB(4 * i + 6, bslot); NEXTSLOT();
      waitv<13>();
      bar();
      LOADA(0, 1, cur, 1);
      loadB(4 * i + 7, bslot); NEXTSLOT();
      waitv<8>();
    }
    bar();
    asm volatile("s_waitcnt vmcnt(0)" : "+v"(tdummy)::"memory");
    bar();
    const int row = mt * 128 + w * 64 + lane;
    float wide = 0.f, bias2 = 0.f; unsigned long long ep = 0, g = 0;
    if (w < 2) {
      ep = __hip_atomic_load(ws + B_ + mt, __ATOMIC_RELAXED, __HIP_MEMORY_SCOPE_AGENT);
      float wd = 0.f;
#pragma unroll
      for (int i = 0; i < ND_; ++i) wd = fmaf(bf2f(xw[row * ND_ + i]), bf2f(ww[i]), wd);
      wide = rbf(rbf(wd) + bf2f(bw[0]));
      bias2 = bf2f(b2[0]);
      g = ((ep * 0x9E3779B97F4A7C15ull + 0x2545F4914F6CDD1Dull) << 25) | ((unsigned long long)nh << 24);
      asm volatile("" : "+v"(wide), "+v"(bias2), "+v"(ep), "+v"(g));
    }
    bar();
    if (w < 2) {
      const f32x4 q = *(const LAS f32x4*)(red + (w * 64 + lane) * 4);
      const unsigned pb = (__float_as_uint((q[0] + q[1]) + (q[2] + q[3])) + 0x80u) >> 8;
      const unsigned long long old = __hip_atomic_exchange(ws + row, g | pb, __ATOMIC_RELAXED, __HIP_MEMORY_SCOPE_AGENT);
      if (((old ^ g) >> 24) == 1ull) {
        const float dp = __uint_as_float(pb << 8) + __uint_as_float(((unsigned)old & 0xFFFFFFu) << 8);
        const float z = rbf(wide + rbf(rbf(dp) + bias2));
        out[row] = (unsigned short)(__float_as_uint(rbf(1.0f / (1.0f + __expf(-z)))) >> 16);
        if (w == 0 && lane == 0) __hip_atomic_store(ws + B_ + mt, ep + 1, __ATOMIC_RELAXED, __HIP_MEMORY_SCOPE_AGENT);
      }
    }
  }
}

extern "C" void kernel_launch(void* const* d_in, const int* in_sizes, int n_in, void* d_out, int out_size, void* d_ws, size_t ws_size,
                              hipStream_t stream) {
  const int V = in_sizes[1] / D_;
  (void)hipFuncSetAttribute((const void*)wd_main, hipFuncAttributeMaxDynamicSharedMemorySize, LDS_BYTES);
  wd_main<<<256, 512, LDS_BYTES, stream>>>((const int*)d_in[0], (const char*)d_in[1], (const char*)d_in[5], (const unsigned short*)d_in[6], (const unsigned short*)d_in[7],
                                           (const unsigned short*)d_in[2], (const unsigned short*)d_in[3], (const unsigned short*)d_in[4], (const unsigned short*)d_in[8],
                                           (__hip_bfloat16*)d_out, (unsigned long long*)d_ws, V, 0);
}
```
